```python
import torch

def _softmax(x, axis=-1):
    return torch.softmax(x, dim=axis)
def _gelu(x):
    return torch.nn.functional.gelu(x, approximate="tanh")
def _silu(x):
    return torch.nn.functional.silu(x)
def _sigmoid(x):
    return torch.sigmoid(x)
def _layernorm(x, axis=-1, eps=1e-5):
    m = x.mean(dim=axis, keepdim=True)
    v = x.var(dim=axis, keepdim=True, unbiased=False)
    return (x - m) / torch.sqrt(v + eps)
def _scatter_add(tgt, idx, src, axis=0):
    return tgt.clone().index_add_(axis, idx, src)
def _segment_sum(src, seg, n):
    out = torch.zeros((n,) + src.shape[1:], dtype=src.dtype)
    return out.index_add_(0, seg, src)
def _pad(x, pw):
    flat = tuple(v for pair in reversed(pw) for v in pair)
    return torch.nn.functional.pad(x, flat)

kernel_name = 'standalone__channel_attn_se__vision'

def setup_inputs():

    _w = (lambda a: (lambda t: t.to(torch.bfloat16) if t.is_floating_point() else t)(torch.as_tensor(a)))
    H = W = 256
    C = 384
    r = max(C // 16, 4)
    return {
        "x": _w((lambda *s: torch.randn(*s, dtype=torch.bfloat16) * 1.6)(1, H, W, C)),
        "w_down": _w((lambda *s: torch.randn(*s, dtype=torch.bfloat16) * 3.2)(C, r)),
        "w_up": _w((lambda *s: torch.randn(*s, dtype=torch.bfloat16) * 1.6)(r, C)),
    }

def reference(x, w_down, w_up):
    s = x.mean(axis=(1, 2))
    h = _silu(torch.einsum('bc,cr->br', s, w_down))
    g = _sigmoid(torch.einsum('br,rc->bc', h, w_up))
    return x * g[:, None, None, :]
```

```cpp
#include <hip/hip_runtime.h>
#include <hip/hip_bf16.h>
#include <stdint.h>

namespace se {
constexpr int kC = 384;
constexpr int kR = 24;
constexpr int kHW = 65536;
constexpr int kWG = 256;
constexpr int kThreads = 768;
constexpr int kIter = (kHW / kWG) / 16;
constexpr int kGroups = 16;
constexpr int kGroupSize = kWG / kGroups;
constexpr int kCntStride = 64;
constexpr int kCntBytes = (kGroups + 1) * kCntStride * 4;
constexpr size_t kPartOff = 8192;
constexpr size_t kGPartOff = kPartOff + (size_t)kWG * kC * 4;
constexpr unsigned kSpinCap = 1u << 22;

typedef __bf16 bf16x2_t __attribute__((ext_vector_type(2)));
typedef float f32x2_t __attribute__((ext_vector_type(2)));
typedef uint32_t u32x4_t __attribute__((ext_vector_type(4)));

__device__ __forceinline__ float lo_f(uint32_t u) { return __uint_as_float(u << 16); }
__device__ __forceinline__ float hi_f(uint32_t u) { return __uint_as_float(u & 0xFFFF0000u); }
__device__ __forceinline__ float bf16_round(float f) { return (float)(__bf16)f; }
__device__ __forceinline__ uint32_t pack_bf16(float a, float b) {
    f32x2_t v = {a, b};
    bf16x2_t r = __builtin_convertvector(v, bf16x2_t);
    return __builtin_bit_cast(uint32_t, r);
}
__device__ __forceinline__ void st_agent(float* p, float v) {
    __hip_atomic_store((unsigned*)p, __float_as_uint(v), __ATOMIC_RELAXED, __HIP_MEMORY_SCOPE_AGENT);
}
__device__ __forceinline__ float ld_agent(const float* p) {
    return __uint_as_float(__hip_atomic_load((const unsigned*)p, __ATOMIC_RELAXED, __HIP_MEMORY_SCOPE_AGENT));
}

__global__ __launch_bounds__(kThreads, 3) void fused(const u32x4_t* __restrict__ x, const uint4* __restrict__ wd,
                                                     const uint4* __restrict__ wu, u32x4_t* __restrict__ out,
                                                     unsigned* cnt, float* part, float* gpart) {
    __shared__ float red[16 * kC];
    __shared__ uint4 wd_s4[kC * kR / 8];
    __shared__ uint4 wu_s4[kR * kC / 8];
    __shared__ float s_s[kC];
    __shared__ float g_s[kC];
    __shared__ float hp[32 * kR];
    __shared__ float h_s[kR];
    __shared__ int last_s;
    const __bf16* wd_s = (const __bf16*)wd_s4;
    const __bf16* wu_s = (const __bf16*)wu_s4;

    const int t = threadIdx.x;
    const int wg = blockIdx.x;
    const int chunk = t % 48;
    const int rowoff = t / 48;

    const u32x4_t* xp = x + (size_t)wg * (kIter * kThreads) + t;
    u32x4_t v[kIter];
#pragma unroll
    for (int i = 0; i < kIter; ++i) v[i] = __builtin_nontemporal_load(xp + i * kThreads);

    for (int i = t; i < kC * kR / 8; i += kThreads) { wd_s4[i] = wd[i]; wu_s4[i] = wu[i]; }

    float acc[8];
#pragma unroll
    for (int k = 0; k < 8; ++k) acc[k] = 0.f;
#pragma unroll
    for (int i = 0; i < kIter; ++i) {
        acc[0] += lo_f(v[i].x); acc[1] += hi_f(v[i].x);
        acc[2] += lo_f(v[i].y); acc[3] += hi_f(v[i].y);
        acc[4] += lo_f(v[i].z); acc[5] += hi_f(v[i].z);
        acc[6] += lo_f(v[i].w); acc[7] += hi_f(v[i].w);
    }
    {
        float4* r4 = (float4*)&red[rowoff * kC + chunk * 8];
        r4[0] = make_float4(acc[0], acc[1], acc[2], acc[3]);
        r4[1] = make_float4(acc[4], acc[5], acc[6], acc[7]);
    }
    __syncthreads();
    const int grp = wg % kGroups;
    if (t < kC) {
        float s = 0.f;
#pragma unroll
        for (int r = 0; r < 16; ++r) s += red[r * kC + t];
        st_agent(&part[(size_t)wg * kC + t], s);
    }
    asm volatile("s_waitcnt vmcnt(0)" ::: "memory");
    __syncthreads();
    if (t == 0) {
        unsigned old = __hip_atomic_fetch_add(&cnt[grp * kCntStride], 1u, __ATOMIC_RELAXED, __HIP_MEMORY_SCOPE_AGENT);
        last_s = (old == kGroupSize - 1);
    }
    __syncthreads();
    if (last_s) {
        if (t < kC) {
            float s = 0.f;
#pragma unroll
            for (int j = 0; j < kGroupSize; ++j) s += ld_agent(&part[(size_t)(grp + j * kGroups) * kC + t]);
            st_agent(&gpart[grp * kC + t], s);
        }
        asm volatile("s_waitcnt vmcnt(0)" ::: "memory");
        __syncthreads();
        if (t == 0) __hip_atomic_fetch_add(&cnt[kGroups * kCntStride], 1u, __ATOMIC_RELAXED, __HIP_MEMORY_SCOPE_AGENT);
    }
    if (t == 0) {
        unsigned spins = 0;
        while (__hip_atomic_load(&cnt[kGroups * kCntStride], __ATOMIC_RELAXED, __HIP_MEMORY_SCOPE_AGENT) < (unsigned)kGroups) {
            __builtin_amdgcn_s_sleep(1);
            if (++spins > kSpinCap) break;
        }
    }
    __syncthreads();
    if (t < kC) {
        float s = 0.f;
#pragma unroll
        for (int g = 0; g < kGroups; ++g) s += ld_agent(&gpart[g * kC + t]);
        s_s[t] = bf16_round(s * (1.0f / kHW));
    }
    __syncthreads();
    {
        const int j = t % kR, p = t / kR;
        float a = 0.f;
#pragma unroll
        for (int i = 0; i < 12; ++i) a += s_s[p * 12 + i] * (float)wd_s[(p * 12 + i) * kR + j];
        hp[p * kR + j] = a;
    }
    __syncthreads();
    if (t < kR) {
        float a = 0.f;
#pragma unroll
        for (int p = 0; p < 32; ++p) a += hp[p * kR + t];
        a = bf16_round(a);
        h_s[t] = bf16_round(a / (1.0f + expf(-a)));
    }
    __syncthreads();
    if (t < kC) {
        float a = 0.f;
#pragma unroll
        for (int j = 0; j < kR; ++j) a += h_s[j] * (float)wu_s[j * kC + t];
        a = bf16_round(a);
        g_s[t] = bf16_round(1.0f / (1.0f + expf(-a)));
    }
    __syncthreads();
    float g[8];
#pragma unroll
    for (int k = 0; k < 8; ++k) g[k] = g_s[chunk * 8 + k];
    u32x4_t* op = out + (size_t)wg * (kIter * kThreads) + t;
#pragma unroll
    for (int i = 0; i < kIter; ++i) {
        u32x4_t o;
        o.x = pack_bf16(lo_f(v[i].x) * g[0], hi_f(v[i].x) * g[1]);
        o.y = pack_bf16(lo_f(v[i].y) * g[2], hi_f(v[i].y) * g[3]);
        o.z = pack_bf16(lo_f(v[i].z) * g[4], hi_f(v[i].z) * g[5]);
        o.w = pack_bf16(lo_f(v[i].w) * g[6], hi_f(v[i].w) * g[7]);
        __builtin_nontemporal_store(o, op + i * kThreads);
    }
}
}

extern "C" void kernel_launch(void* const* d_in, const int* in_sizes, int n_in,
                              void* d_out, int out_size, void* d_ws, size_t ws_size,
                              hipStream_t stream) {
    using namespace se;
    const u32x4_t* x = (const u32x4_t*)d_in[0];
    const uint4* wd = (const uint4*)d_in[1];
    const uint4* wu = (const uint4*)d_in[2];
    char* ws = (char*)d_ws;
    (void)hipMemsetAsync(ws, 0, kCntBytes, stream);
    fused<<<kWG, kThreads, 0, stream>>>(x, wd, wu, (u32x4_t*)d_out, (unsigned*)ws, (float*)(ws + kPartOff),
                                        (float*)(ws + kGPartOff));
}
```

```cpp
#include <hip/hip_runtime.h>
#include <hip/hip_bf16.h>
#include <stdint.h>

namespace se {
#define STAMP(k)
#define STAMP_DECL
#define STAMP_FLUSH
constexpr int kC = 384;
constexpr int kR = 24;
constexpr int kHW = 65536;
constexpr int kWG = 256;
constexpr int kThreads = 768;
constexpr int kIter = (kHW / kWG) / 16;
constexpr int kGroups = 16;
constexpr int kGroupSize = kWG / kGroups;
constexpr int kOwn = kC / kGroupSize;
constexpr size_t kPartBytes = (size_t)kWG * kC * 8;
constexpr size_t kGPartBytes = (size_t)kGroups * kC * 8;
constexpr unsigned kSpinCap = 1u << 20;
constexpr unsigned long long kTag = 1ull << 32;

typedef __bf16 bf16x2_t __attribute__((ext_vector_type(2)));
typedef float f32x2_t __attribute__((ext_vector_type(2)));
typedef uint32_t u32x4_t __attribute__((ext_vector_type(4)));
typedef unsigned long long u64;

__device__ __forceinline__ float lo_f(uint32_t u) { return __uint_as_float(u << 16); }
__device__ __forceinline__ float hi_f(uint32_t u) { return __uint_as_float(u & 0xFFFF0000u); }
__device__ __forceinline__ float bf16_round(float f) { return (float)(__bf16)f; }
__device__ __forceinline__ uint32_t pack_bf16(float a, float b) {
    f32x2_t v = {a, b};
    bf16x2_t r = __builtin_convertvector(v, bf16x2_t);
    return __builtin_bit_cast(uint32_t, r);
}
__device__ __forceinline__ void st_granule(u64* p, float v) {
    __hip_atomic_store(p, kTag | (u64)__float_as_uint(v), __ATOMIC_RELAXED, __HIP_MEMORY_SCOPE_AGENT);
}
__device__ __forceinline__ u64 ld_granule(const u64* p) {
    return __hip_atomic_load(p, __ATOMIC_RELAXED, __HIP_MEMORY_SCOPE_AGENT);
}

__global__ __launch_bounds__(kThreads, 3) void fused(const u32x4_t* __restrict__ x, const uint4* __restrict__ wd,
                                                     const uint4* __restrict__ wu, u32x4_t* __restrict__ out,
                                                     u64* part, u64* gpart) {
    __shared__ float red[16 * kC];
    __shared__ uint4 wd_s4[kC * kR / 8];
    __shared__ uint4 wu_s4[kR * kC / 8];
    __shared__ float s_s[kC];
    __shared__ float hp[12 * kR];
    __shared__ float h_s[kR];
    const __bf16* wd_s = (const __bf16*)wd_s4;

    STAMP_DECL
    const int t = threadIdx.x;
    const int wg = blockIdx.x;
    const int chunk = t % 48;
    const int rowoff = t / 48;
    STAMP(0);
    const u32x4_t* xp = x + (size_t)wg * (kIter * kThreads) + t;
    u32x4_t v[kIter];
#pragma unroll
    for (int i = 0; i < kIter; ++i) v[i] = __builtin_nontemporal_load(xp + i * kThreads);

    for (int i = t; i < kC * kR / 8; i += kThreads) { wd_s4[i] = wd[i]; wu_s4[i] = wu[i]; }

    float acc[8];
#pragma unroll
    for (int k = 0; k < 8; ++k) acc[k] = 0.f;
#pragma unroll
    for (int i = 0; i < kIter; ++i) {
        acc[0] += lo_f(v[i].x); acc[1] += hi_f(v[i].x);
        acc[2] += lo_f(v[i].y); acc[3] += hi_f(v[i].y);
        acc[4] += lo_f(v[i].z); acc[5] += hi_f(v[i].z);
        acc[6] += lo_f(v[i].w); acc[7] += hi_f(v[i].w);
    }
#pragma unroll
    for (int i = 0; i < kIter; ++i) asm volatile("" : "+v"(v[i]));
    {
        float4* r4 = (float4*)&red[rowoff * kC + chunk * 8];
        r4[0] = make_float4(acc[0], acc[1], acc[2], acc[3]);
        r4[1] = make_float4(acc[4], acc[5], acc[6], acc[7]);
    }
    __syncthreads();
    STAMP(1);
    const int grp = wg % kGroups, mem = wg / kGroups;
    if (t < kC) {
        float s = 0.f;
#pragma unroll
        for (int r = 0; r < 16; ++r) s += red[r * kC + t];
        st_granule(&part[(size_t)wg * kC + t], s);
        const u64* src = &part[(size_t)(grp + (t / kOwn) * kGroups) * kC + mem * kOwn + (t % kOwn)];
        u64 gv = ld_granule(src);
        for (unsigned spins = 0; (gv >> 32) != 1ull && spins < kSpinCap; ++spins) {
            __builtin_amdgcn_s_sleep(1);
            gv = ld_granule(src);
        }
        red[t] = __uint_as_float((uint32_t)gv);
    }
    __syncthreads();
    if (t < kOwn) {
        float s = 0.f;
#pragma unroll
        for (int j = 0; j < kGroupSize; ++j) s += red[j * kOwn + t];
        st_granule(&gpart[(size_t)grp * kC + mem * kOwn + t], s);
    }
    STAMP(2);
    {
        const int c = t % kC, hf = t / kC;
        u64 gv[8];
        bool ok;
        unsigned spins = 0;
        do {
            ok = true;
#pragma unroll
            for (int g = 0; g < 8; ++g) { gv[g] = ld_granule(&gpart[(size_t)(hf * 8 + g) * kC + c]); ok &= (gv[g] >> 32) == 1ull; }
            if (!ok) __builtin_amdgcn_s_sleep(1);
        } while (!ok && ++spins < kSpinCap);
        float s = 0.f;
#pragma unroll
        for (int g = 0; g < 8; ++g) s += __uint_as_float((uint32_t)gv[g]);
        red[1024 + t] = s;
    }
    __syncthreads();
    if (t < kC) s_s[t] = bf16_round((red[1024 + t] + red[1024 + kC + t]) * (1.0f / kHW));
    __syncthreads();
    STAMP(3);
    {
        const int w = t >> 6, l = t & 63;
        if (l < 48) {
            const int j = l % kR, half = l / kR;
            const int c0 = w * 32 + half * 16;
            float a = 0.f;
#pragma unroll
            for (int i = 0; i < 16; ++i) a += s_s[c0 + i] * (float)wd_s[(c0 + i) * kR + j];
            red[(w * 2 + half) * kR + j] = a;
        }
    }
    __syncthreads();
    if (t < kR) {
        float a = 0.f;
#pragma unroll
        for (int p = 0; p < 24; ++p) a += red[p * kR + t];
        a = bf16_round(a);
        h_s[t] = bf16_round(a / (1.0f + expf(-a)));
    }
    __syncthreads();
    float g[8];
#pragma unroll
    for (int k = 0; k < 8; ++k) g[k] = 0.f;
#pragma unroll
    for (int j = 0; j < kR; ++j) {
        const uint4 wv = wu_s4[(j * kC + chunk * 8) / 8];
        const float hj = h_s[j];
        g[0] += hj * lo_f(wv.x); g[1] += hj * hi_f(wv.x);
        g[2] += hj * lo_f(wv.y); g[3] += hj * hi_f(wv.y);
        g[4] += hj * lo_f(wv.z); g[5] += hj * hi_f(wv.z);
        g[6] += hj * lo_f(wv.w); g[7] += hj * hi_f(wv.w);
    }
#pragma unroll
    for (int k = 0; k < 8; ++k) { const float a = bf16_round(g[k]); g[k] = bf16_round(1.0f / (1.0f + expf(-a))); }
    STAMP(4);
    u32x4_t* op = out + (size_t)wg * (kIter * kThreads) + t;
#pragma unroll
    for (int i = 0; i < kIter; ++i) {
        u32x4_t o;
        o.x = pack_bf16(lo_f(v[i].x) * g[0], hi_f(v[i].x) * g[1]);
        o.y = pack_bf16(lo_f(v[i].y) * g[2], hi_f(v[i].y) * g[3]);
        o.z = pack_bf16(lo_f(v[i].z) * g[4], hi_f(v[i].z) * g[5]);
        o.w = pack_bf16(lo_f(v[i].w) * g[6], hi_f(v[i].w) * g[7]);
        op[i * kThreads] = o;
    }
    STAMP(5);
    STAMP(6);
    STAMP(7);
    STAMP_FLUSH;
}
}

extern "C" void kernel_launch(void* const* d_in, const int* in_sizes, int n_in,
                              void* d_out, int out_size, void* d_ws, size_t ws_size,
                              hipStream_t stream) {
    using namespace se;
    const u32x4_t* x = (const u32x4_t*)d_in[0];
    const uint4* wd = (const uint4*)d_in[1];
    const uint4* wu = (const uint4*)d_in[2];
    char* ws = (char*)d_ws;
    (void)hipMemsetAsync(ws, 0, kPartBytes + kGPartBytes, stream);
    fused<<<kWG, kThreads, 0, stream>>>(x, wd, wu, (u32x4_t*)d_out, (u64*)ws, (u64*)(ws + kPartBytes));
}
```

```cpp
#include <hip/hip_runtime.h>
#include <hip/hip_bf16.h>
#include <stdint.h>

namespace se {
constexpr int kC = 384;
constexpr int kR = 24;
constexpr int kHW = 65536;
constexpr int kWG = 256;
constexpr int kThreads = 768;
constexpr int kIter = (kHW / kWG) / 16;
constexpr int kGroups = 16;
constexpr int kGroupSize = kWG / kGroups;
constexpr int kOwn = kC / kGroupSize;
constexpr size_t kPartBytes = (size_t)kWG * kC * 8;
constexpr size_t kGPartBytes = (size_t)kGroups * kC * 8;
constexpr unsigned kSpinCap = 1u << 20;
constexpr unsigned long long kTag = 1ull << 32;

typedef __bf16 bf16x2_t __attribute__((ext_vector_type(2)));
typedef float f32x2_t __attribute__((ext_vector_type(2)));
typedef uint32_t u32x4_t __attribute__((ext_vector_type(4)));
typedef unsigned long long u64;

__device__ __forceinline__ float lo_f(uint32_t u) { return __uint_as_float(u << 16); }
__device__ __forceinline__ float hi_f(uint32_t u) { return __uint_as_float(u & 0xFFFF0000u); }
__device__ __forceinline__ float bf16_round(float f) { return (float)(__bf16)f; }
__device__ __forceinline__ uint32_t pack_bf16(float a, float b) {
    f32x2_t v = {a, b};
    bf16x2_t r = __builtin_convertvector(v, bf16x2_t);
    return __builtin_bit_cast(uint32_t, r);
}
__device__ __forceinline__ void st_granule(u64* p, float v) {
    __hip_atomic_store(p, kTag | (u64)__float_as_uint(v), __ATOMIC_RELAXED, __HIP_MEMORY_SCOPE_AGENT);
}
__device__ __forceinline__ u64 ld_granule(const u64* p) {
    return __hip_atomic_load(p, __ATOMIC_RELAXED, __HIP_MEMORY_SCOPE_AGENT);
}

__global__ __launch_bounds__(kThreads, 3) void fused(const u32x4_t* __restrict__ x, const uint4* __restrict__ wd,
                                                     const uint4* __restrict__ wu, u32x4_t* __restrict__ out,
                                                     u64* part, u64* gpart) {
    __shared__ float red[16 * kC];
    __shared__ uint4 wd_s4[kC * kR / 8];
    __shared__ uint4 wu_s4[kR * kC / 8];
    __shared__ __attribute__((aligned(16))) float s_s[kC];
    __shared__ float hp[12 * kR];
    __shared__ float h_s[kR];
    const __bf16* wd_s = (const __bf16*)wd_s4;
    const __bf16* wu_s = (const __bf16*)wu_s4;

    const int t = threadIdx.x;
    const int wg = blockIdx.x;
    const int chunk = t % 48;
    const int rowoff = t / 48;
    const u32x4_t* xp = x + (size_t)wg * (kIter * kThreads) + t;
    u32x4_t v[kIter];
#pragma unroll
    for (int i = 0; i < kIter; ++i) v[i] = __builtin_nontemporal_load(xp + i * kThreads);

    for (int i = t; i < kC * kR / 8; i += kThreads) { wd_s4[i] = wd[i]; wu_s4[i] = wu[i]; }

    float acc[8];
#pragma unroll
    for (int k = 0; k < 8; ++k) acc[k] = 0.f;
#pragma unroll
    for (int i = 0; i < kIter; ++i) {
        acc[0] += lo_f(v[i].x); acc[1] += hi_f(v[i].x);
        acc[2] += lo_f(v[i].y); acc[3] += hi_f(v[i].y);
        acc[4] += lo_f(v[i].z); acc[5] += hi_f(v[i].z);
        acc[6] += lo_f(v[i].w); acc[7] += hi_f(v[i].w);
    }
#pragma unroll
    for (int i = 0; i < kIter; ++i) asm volatile("" : "+v"(v[i]));
    {
        float4* r4 = (float4*)&red[rowoff * kC + chunk * 8];
        r4[0] = make_float4(acc[0], acc[1], acc[2], acc[3]);
        r4[1] = make_float4(acc[4], acc[5], acc[6], acc[7]);
    }
    __syncthreads();
    const int grp = wg % kGroups, mem = wg / kGroups;
    if (t < kC) {
        float s = 0.f;
#pragma unroll
        for (int r = 0; r < 16; ++r) s += red[r * kC + t];
        st_granule(&part[(size_t)wg * kC + t], s);
        const u64* src = &part[(size_t)(grp + (t / kOwn) * kGroups) * kC + mem * kOwn + (t % kOwn)];
        u64 gv = ld_granule(src);
        for (unsigned spins = 0; (gv >> 32) != 1ull && spins < kSpinCap; ++spins) {
            __builtin_amdgcn_s_sleep(1);
            gv = ld_granule(src);
        }
        red[t] = __uint_as_float((uint32_t)gv);
    }
    __syncthreads();
    if (t < kOwn) {
        float s = 0.f;
#pragma unroll
        for (int j = 0; j < kGroupSize; ++j) s += red[j * kOwn + t];
        st_granule(&gpart[(size_t)grp * kC + mem * kOwn + t], s);
    }
    {
        const int c = t % kC, hf = t / kC;
        u64 gv[8];
        bool ok;
        unsigned spins = 0;
        do {
            ok = true;
#pragma unroll
            for (int g = 0; g < 8; ++g) { gv[g] = ld_granule(&gpart[(size_t)(hf * 8 + g) * kC + c]); ok &= (gv[g] >> 32) == 1ull; }
            if (!ok) __builtin_amdgcn_s_sleep(1);
        } while (!ok && ++spins < kSpinCap);
        float s = 0.f;
#pragma unroll
        for (int g = 0; g < 8; ++g) s += __uint_as_float((uint32_t)gv[g]);
        red[1024 + t] = s;
    }
    __syncthreads();
    if (t < kC) s_s[t] = bf16_round((red[1024 + t] + red[1024 + kC + t]) * (1.0f / kHW));
    __syncthreads();
    {
        const int w = t >> 6, l = t & 63;
        if (l < 48) {
            const int j = l % kR, half = l / kR;
            const int c0 = w * 32 + half * 16;
            float a = 0.f;
#pragma unroll
            for (int i = 0; i < 16; ++i) a += s_s[c0 + i] * (float)wd_s[(c0 + i) * kR + j];
            red[(w * 2 + half) * kR + j] = a;
        }
    }
    __syncthreads();
    if (t < kR) {
        float a = 0.f;
#pragma unroll
        for (int p = 0; p < 24; ++p) a += red[p * kR + t];
        a = bf16_round(a);
        h_s[t] = bf16_round(a / (1.0f + expf(-a)));
    }
    __syncthreads();
    if (t < kC) {
        float a = 0.f;
#pragma unroll
        for (int j = 0; j < kR; ++j) a += h_s[j] * (float)wu_s[j * kC + t];
        a = bf16_round(a);
        s_s[t] = bf16_round(1.0f / (1.0f + expf(-a)));
    }
    __syncthreads();
    float g[8];
    {
        const float4 g0 = *(const float4*)&s_s[chunk * 8], g1 = *(const float4*)&s_s[chunk * 8 + 4];
        g[0] = g0.x; g[1] = g0.y; g[2] = g0.z; g[3] = g0.w; g[4] = g1.x; g[5] = g1.y; g[6] = g1.z; g[7] = g1.w;
    }
    u32x4_t* op = out + (size_t)wg * (kIter * kThreads) + t;
#pragma unroll
    for (int i = 0; i < kIter; ++i) {
        u32x4_t o;
        o.x = pack_bf16(lo_f(v[i].x) * g[0], hi_f(v[i].x) * g[1]);
        o.y = pack_bf16(lo_f(v[i].y) * g[2], hi_f(v[i].y) * g[3]);
        o.z = pack_bf16(lo_f(v[i].z) * g[4], hi_f(v[i].z) * g[5]);
        o.w = pack_bf16(lo_f(v[i].w) * g[6], hi_f(v[i].w) * g[7]);
        __builtin_nontemporal_store(o, op + i * kThreads);
    }
}
}

extern "C" void kernel_launch(void* const* d_in, const int* in_sizes, int n_in,
                              void* d_out, int out_size, void* d_ws, size_t ws_size,
                              hipStream_t stream) {
    using namespace se;
    const u32x4_t* x = (const u32x4_t*)d_in[0];
    const uint4* wd = (const uint4*)d_in[1];
    const uint4* wu = (const uint4*)d_in[2];
    char* ws = (char*)d_ws;
    (void)hipMemsetAsync(ws, 0, kPartBytes + kGPartBytes, stream);
    fused<<<kWG, kThreads, 0, stream>>>(x, wd, wu, (u32x4_t*)d_out, (u64*)ws, (u64*)(ws + kPartBytes));
}
```
